```python
import torch

def _softmax(x, axis=-1):
    return torch.softmax(x, dim=axis)
def _gelu(x):
    return torch.nn.functional.gelu(x, approximate="tanh")
def _silu(x):
    return torch.nn.functional.silu(x)
def _sigmoid(x):
    return torch.sigmoid(x)
def _layernorm(x, axis=-1, eps=1e-5):
    m = x.mean(dim=axis, keepdim=True)
    v = x.var(dim=axis, keepdim=True, unbiased=False)
    return (x - m) / torch.sqrt(v + eps)
def _scatter_add(tgt, idx, src, axis=0):
    return tgt.clone().index_add_(axis, idx, src)
def _segment_sum(src, seg, n):
    out = torch.zeros((n,) + src.shape[1:], dtype=src.dtype)
    return out.index_add_(0, seg, src)
def _pad(x, pw):
    flat = tuple(v for pair in reversed(pw) for v in pair)
    return torch.nn.functional.pad(x, flat)

kernel_name = 'concat_heads__mha_block_cached__gqa_block_cached__near_ridge'

def setup_inputs():

    _w = (lambda a: (lambda t: t.to(torch.bfloat16) if t.is_floating_point() else t)(torch.as_tensor(a)))
    def _A_setup():
        B, Sq, D = 8, 96, 448
        H, Sk, Dh = 16, 512, 256
        return {
            "x": _w((lambda *s: torch.randn(*s, dtype=torch.bfloat16) * 0.8)(B, Sq, D)),
            "k_cache": _w((lambda *s: torch.randn(*s, dtype=torch.bfloat16) * 0.8)(B, H, Sk, Dh)),
            "v_cache": _w((lambda *s: torch.randn(*s, dtype=torch.bfloat16) * 0.05)(B, H, Sk, Dh)),
            "wq": _w((lambda *s: torch.randn(*s, dtype=torch.bfloat16) * 0.8)(D, H * Dh) * (D ** -0.5)),
            "wo": _w((lambda *s: torch.randn(*s, dtype=torch.bfloat16) * 0.05)(H * Dh, D) * ((H * Dh) ** -0.5)),
        }
    _A_in = _A_setup()

    def _B_setup():
        B, Sq, D = 8, 96, 448
        Hq, Hkv, Sk, Dh = 16, 16, 512, 256
        assert Hq % Hkv == 0, (Hq, Hkv)
        return {
            "x": _w((lambda *s: torch.randn(*s, dtype=torch.bfloat16) * 0.8)(B, Sq, D)),
            "k_cache": _w((lambda *s: torch.randn(*s, dtype=torch.bfloat16) * 0.8)(B, Hkv, Sk, Dh)),
            "v_cache": _w((lambda *s: torch.randn(*s, dtype=torch.bfloat16) * 0.05)(B, Hkv, Sk, Dh)),
            "wq": _w((lambda *s: torch.randn(*s, dtype=torch.bfloat16) * 0.8)(D, Hq * Dh) * (D ** -0.5)),
            "wo": _w((lambda *s: torch.randn(*s, dtype=torch.bfloat16) * 0.05)(Hq * Dh, D) * ((Hq * Dh) ** -0.5)),
        }
    _B_in = _B_setup()

    return {
        "A_x": _A_in["x"],
        "A_k_cache": _A_in["k_cache"],
        "A_v_cache": _A_in["v_cache"],
        "A_wq": _A_in["wq"],
        "A_wo": _A_in["wo"],
        "B_x": _B_in["x"],
        "B_k_cache": _B_in["k_cache"],
        "B_v_cache": _B_in["v_cache"],
        "B_wq": _B_in["wq"],
        "B_wo": _B_in["wo"],
    }

def reference(A_x, A_k_cache, A_v_cache, A_wq, A_wo, B_x, B_k_cache, B_v_cache, B_wq, B_wo):
    def _A_ref(x, k_cache, v_cache, wq, wo):
        B, Sq, D = x.shape
        H, Dh = 16, 256
        q = (lambda x, *d: x.permute(*d))((x @ wq).reshape(B, Sq, H, Dh), 0, 2, 1, 3)
        scores = torch.einsum("bhqd,bhkd->bhqk", q, k_cache) * (Dh ** -0.5)
        p = _softmax(scores, axis=-1)
        o = torch.einsum("bhqk,bhkd->bhqd", p, v_cache)
        return (lambda x, *d: x.permute(*d))(o, 0, 2, 1, 3).reshape(B, Sq, H * Dh) @ wo

    def _B_ref(x, k_cache, v_cache, wq, wo):
        B, Sq, D = x.shape
        Hq, Dh = 16, 256
        Hkv = k_cache.shape[1]
        rep = Hq // Hkv
        q = (lambda x, *d: x.permute(*d))((x @ wq).reshape(B, Sq, Hq, Dh), 0, 2, 1, 3)
        k = (lambda x, n, axis: torch.repeat_interleave(x, n, dim=axis))(k_cache, rep, axis=1)
        v = (lambda x, n, axis: torch.repeat_interleave(x, n, dim=axis))(v_cache, rep, axis=1)
        scores = torch.einsum("bhqd,bhkd->bhqk", q, k) * (Dh ** -0.5)
        p = _softmax(scores, axis=-1)
        o = torch.einsum("bhqk,bhkd->bhqd", p, v)
        return (lambda x, *d: x.permute(*d))(o, 0, 2, 1, 3).reshape(B, Sq, Hq * Dh) @ wo

    a = _A_ref(A_x, A_k_cache, A_v_cache, A_wq, A_wo)
    b = _B_ref(B_x, B_k_cache, B_v_cache, B_wq, B_wo)
    return torch.concatenate([a, b], axis=-1)
```

```cpp
#include <hip/hip_runtime.h>
#include <hip/hip_bf16.h>
typedef __hip_bfloat16 bf16;
__device__ inline float b2f(bf16 v){return __bfloat162float(v);}
__global__ void qproj(const bf16* x,const bf16* wq, bf16* q){
  int col = blockIdx.x*256+threadIdx.x; int row=blockIdx.y;
  float acc=0; for(int k=0;k<448;k++) acc+=b2f(x[row*448+k])*b2f(wq[(size_t)k*4096+col]);
  q[(size_t)row*4096+col]=__float2bfloat16(acc);
}
__global__ void attn(const bf16* q,const bf16* K,const bf16* V, bf16* o){
  int qi=blockIdx.x, h=blockIdx.y, b=blockIdx.z; int t=threadIdx.x;
  __shared__ float qs[256]; __shared__ float s[512]; __shared__ float red[256];
  qs[t]=b2f(q[(size_t)(b*96+qi)*4096+h*256+t]); __syncthreads();
  const bf16* Kb=K+(size_t)(b*16+h)*512*256; const bf16* Vb=V+(size_t)(b*16+h)*512*256;
  for(int kk=t;kk<512;kk+=256){ float a=0; for(int d=0;d<256;d++) a+=qs[d]*b2f(Kb[kk*256+d]);
    a=b2f(__float2bfloat16(a)); a=b2f(__float2bfloat16(a*0.0625f)); s[kk]=a; }
  __syncthreads();
  float m=fmaxf(s[t],s[t+256]); red[t]=m; __syncthreads();
  for(int o2=128;o2>0;o2>>=1){ if(t<o2) red[t]=fmaxf(red[t],red[t+o2]); __syncthreads(); }
  m=red[0]; __syncthreads();
  float e0=__expf(s[t]-m), e1=__expf(s[t+256]-m); red[t]=e0+e1; __syncthreads();
  for(int o2=128;o2>0;o2>>=1){ if(t<o2) red[t]+=red[t+o2]; __syncthreads(); }
  float l=red[0]; __syncthreads();
  s[t]=b2f(__float2bfloat16(e0/l)); s[t+256]=b2f(__float2bfloat16(e1/l)); __syncthreads();
  float a=0; for(int kk=0;kk<512;kk++) a+=s[kk]*b2f(Vb[kk*256+t]);
  o[(size_t)(b*96+qi)*4096+h*256+t]=__float2bfloat16(a);
}
__global__ void oproj(const bf16* o,const bf16* wo, bf16* out,int off){
  int col=threadIdx.x; int row=blockIdx.x; if(col>=448) return;
  float acc=0; for(int k=0;k<4096;k++) acc+=b2f(o[(size_t)row*4096+k])*b2f(wo[(size_t)k*448+col]);
  out[(size_t)row*896+off+col]=__float2bfloat16(acc);
}
extern "C" void kernel_launch(void* const* d_in, const int* in_sizes, int n_in, void* d_out, int out_size, void* d_ws, size_t ws_size, hipStream_t stream){
  bf16* q=(bf16*)d_ws; bf16* o=q+768*4096;
  for(int br=0;br<2;br++){
    const bf16* x=(const bf16*)d_in[br*5+0]; const bf16* K=(const bf16*)d_in[br*5+1]; const bf16* V=(const bf16*)d_in[br*5+2];
    const bf16* wq=(const bf16*)d_in[br*5+3]; const bf16* wo=(const bf16*)d_in[br*5+4];
    qproj<<<dim3(16,768),256,0,stream>>>(x,wq,q);
    attn<<<dim3(96,16,8),256,0,stream>>>(q,K,V,o);
    oproj<<<768,448,0,stream>>>(o,wo,(bf16*)d_out,br*448);
  }
}
```

```cpp
#include <hip/hip_runtime.h>
#include <stdint.h>

#define DI __device__ __forceinline__
#define LAS __attribute__((address_space(3)))
#define SB() __builtin_amdgcn_sched_barrier(0)
typedef __bf16 bf16x8 __attribute__((ext_vector_type(8)));
typedef __bf16 bf16x4 __attribute__((ext_vector_type(4)));
typedef __bf16 bf16x2 __attribute__((ext_vector_type(2)));
typedef float f32x16 __attribute__((ext_vector_type(16)));
typedef float f32x2 __attribute__((ext_vector_type(2)));
typedef unsigned u32x4 __attribute__((ext_vector_type(4)));

#define KV_NT 1
#define W_NT 1

constexpr int SLOT = 16384;
constexpr int LDS_BYTES = 163840;

struct Params {
  const void* x[2]; const void* k[2]; const void* v[2]; const void* wq[2]; const void* wo[2];
  void* out; void* ws;
};

DI unsigned lane_id() { unsigned l; asm volatile("v_mbcnt_lo_u32_b32 %0, -1, 0\n\tv_mbcnt_hi_u32_b32 %0, -1, %0" : "=v"(l)); return l; }
DI unsigned rfl(unsigned v) { return __builtin_amdgcn_readfirstlane(v); }
DI u32x4 make_srd(const void* p, unsigned bytes) {
  unsigned long long a = (unsigned long long)p; u32x4 s;
  s[0] = rfl((unsigned)a); s[1] = rfl((unsigned)(a >> 32)) & 0xffffu; s[2] = bytes; s[3] = 0x00020000u; return s;
}
template <int NT> DI void dma4(unsigned m0v, unsigned v0, unsigned v1, unsigned v2, unsigned v3, u32x4 srd, unsigned soff) {
  if constexpr (NT)
    asm volatile("s_mov_b32 m0, %0\n\ts_nop 0\n\t"
                 "buffer_load_dwordx4 %1, %5, %6 offen nt lds\n\t"
                 "buffer_load_dwordx4 %2, %5, %6 offen offset:1024 nt lds\n\t"
                 "buffer_load_dwordx4 %3, %5, %6 offen offset:2048 nt lds\n\t"
                 "buffer_load_dwordx4 %4, %5, %6 offen offset:3072 nt lds"
                 :: "s"(m0v), "v"(v0), "v"(v1), "v"(v2), "v"(v3), "s"(srd), "s"(soff) : "memory");
  else
    asm volatile("s_mov_b32 m0, %0\n\ts_nop 0\n\t"
                 "buffer_load_dwordx4 %1, %5, %6 offen lds\n\t"
                 "buffer_load_dwordx4 %2, %5, %6 offen offset:1024 lds\n\t"
                 "buffer_load_dwordx4 %3, %5, %6 offen offset:2048 lds\n\t"
                 "buffer_load_dwordx4 %4, %5, %6 offen offset:3072 lds"
                 :: "s"(m0v), "v"(v0), "v"(v1), "v"(v2), "v"(v3), "s"(srd), "s"(soff) : "memory");
}
template <int NT> DI void dma1(unsigned m0v, unsigned v0, u32x4 srd, unsigned soff) {
  if constexpr (NT)
    asm volatile("s_mov_b32 m0, %0\n\ts_nop 0\n\tbuffer_load_dwordx4 %1, %2, %3 offen nt lds" :: "s"(m0v), "v"(v0), "s"(srd), "s"(soff) : "memory");
  else
    asm volatile("s_mov_b32 m0, %0\n\ts_nop 0\n\tbuffer_load_dwordx4 %1, %2, %3 offen lds" :: "s"(m0v), "v"(v0), "s"(srd), "s"(soff) : "memory");
}
template <int N> DI void waitv() { asm volatile("s_waitcnt vmcnt(%0)" :: "i"(N) : "memory"); }
DI void bar() { asm volatile("s_waitcnt lgkmcnt(0)\n\ts_barrier" ::: "memory"); }
DI void bar_only() { asm volatile("s_barrier" ::: "memory"); }
DI bf16x8 trA(const LAS char* p0, const LAS char* p1) {
  bf16x4 a = __builtin_amdgcn_ds_read_tr16_b64_v4bf16((LAS bf16x4*)p0);
  bf16x4 b = __builtin_amdgcn_ds_read_tr16_b64_v4bf16((LAS bf16x4*)p1);
  return __builtin_shufflevector(a, b, 0, 1, 2, 3, 4, 5, 6, 7);
}
DI f32x16 mfma(bf16x8 a, bf16x8 b, f32x16 c) { return __builtin_amdgcn_mfma_f32_32x32x16_bf16(a, b, c, 0, 0, 0); }
DI unsigned cvtpk(float lo, float hi) { f32x2 v = {lo, hi}; bf16x2 b = __builtin_convertvector(v, bf16x2); return __builtin_bit_cast(unsigned, b); }
DI bf16x8 pack8(const f32x16& a, int s, float sc) {
  u32x4 w;
  w[0] = cvtpk(a[8 * s + 0] * sc, a[8 * s + 1] * sc); w[1] = cvtpk(a[8 * s + 2] * sc, a[8 * s + 3] * sc);
  w[2] = cvtpk(a[8 * s + 4] * sc, a[8 * s + 5] * sc); w[3] = cvtpk(a[8 * s + 6] * sc, a[8 * s + 7] * sc);
  return __builtin_bit_cast(bf16x8, w);
}
DI bf16x8 pack8n(const f32x16& a, int s) {
  u32x4 w;
  w[0] = cvtpk(a[8 * s + 0], a[8 * s + 1]); w[1] = cvtpk(a[8 * s + 2], a[8 * s + 3]);
  w[2] = cvtpk(a[8 * s + 4], a[8 * s + 5]); w[3] = cvtpk(a[8 * s + 6], a[8 * s + 7]);
  return __builtin_bit_cast(bf16x8, w);
}

DI constexpr int slot_of(int m) { return m < 14 ? 6 + ((m + 2) & 3) : (m - 14) % 10; }

__global__ __launch_bounds__(256, 1) void attn_kernel(Params p) {
  extern __shared__ __attribute__((aligned(16))) char smem[];
  const int u = blockIdx.x;
  const int xcd = u & 7, idx = u >> 3;
  const int br = xcd >> 2, h = 4 * (xcd & 3) + (idx & 3), b = idx >> 2;
  const int wave = rfl(threadIdx.x >> 6);
  const unsigned lds0 = (unsigned)(size_t)(LAS char*)smem;
  const size_t kvoff = (size_t)(b * 16 + h) * (512 * 256 * 2);
  const int rot = (b * 14) >> 3;

  if (wave == 3) {
    const unsigned lane = lane_id();
    const u32x4 srdW = make_srd((const char*)p.wq[br] + h * 512, 448u * 8192u - h * 512u);
    const u32x4 srdK = make_srd((const char*)p.k[br] + kvoff, 262144u);
    const u32x4 srdV = make_srd((const char*)p.v[br] + kvoff, 262144u);
    unsigned vW[4], vK[8], vV[4];
    { const unsigned hi = lane >> 5, pc = lane & 31;
      #pragma unroll
      for (int i = 0; i < 4; ++i) { const unsigned r3 = (2 * (i & 1) + hi) & 3; vW[i] = hi * 8192u + ((pc ^ (r3 << 2)) << 4) + i * (16384u - 1024u); }
      #pragma unroll
      for (int i = 0; i < 8; ++i) vK[i] = hi * 512u + ((pc ^ ((2 * i + hi) & 15)) << 4);
      const unsigned r8 = lane >> 3, c8 = lane & 7;
      #pragma unroll
      for (int i = 0; i < 4; ++i) vV[i] = r8 * 512u + ((c8 ^ (((r8 >> 1) & 1) << 2)) << 4) + i * (4096u - 1024u);
    }
    auto issueW = [&](int n) {
      int it = n + rot; if (it >= 14) it -= 14;
      const unsigned m0 = lds0 + (6 + ((n + 2) & 3)) * SLOT, so = it * (32u * 8192u);
      #pragma unroll
      for (int g = 0; g < 4; ++g) dma4<W_NT>(m0 + g * 4096, vW[0], vW[1], vW[2], vW[3], srdW, so + g * 4 * 16384u);
    };
    auto issueK = [&](int i, int slot) {
      const unsigned m0 = lds0 + slot * SLOT, so = i * 16384u;
      #pragma unroll
      for (int g = 0; g < 4; ++g) dma4<KV_NT>(m0 + g * 4096, vK[(4 * g) & 7], vK[(4 * g + 1) & 7], vK[(4 * g + 2) & 7], vK[(4 * g + 3) & 7], srdK, so + g * 4096);
    };
    auto issueV = [&](int i, int slot) {
      const unsigned m0 = lds0 + slot * SLOT, so = (i & 3) * 65536u + (i >> 2) * 128u;
      #pragma unroll
      for (int g = 0; g < 4; ++g) dma4<KV_NT>(m0 + g * 4096, vV[0], vV[1], vV[2], vV[3], srdV, so + g * 4 * 4096u);
    };
    issueW(0); issueW(1); issueW(2); issueW(3);
    waitv<48>(); bar_only();
    #pragma unroll 1
    for (int n = 1; n < 14; ++n) {
      waitv<32>(); bar_only();
      waitv<47>();
      if (n <= 10) issueW(n + 3); else issueK(n - 5, 6 + ((n + 1) & 3));
    }
    int slot = 9;
    #pragma unroll 1
    for (int n = 14; n < 42; ++n) {
      bar_only();
      if (n <= 36) {
        waitv<47>();
        if (n <= 20) issueK(n - 5, slot); else issueV(n - 21, slot);
      }
      slot = slot == 9 ? 0 : slot + 1;
    }
    waitv<48>(); bar_only(); waitv<32>(); bar_only(); waitv<16>(); bar_only(); waitv<0>(); bar_only();
    return;
  }

  const int qg = wave;
  const unsigned lane = lane_id();
  bf16x8 xf[28];
  {
    const unsigned r = lane & 31, hh = lane >> 5;
    const char* xp = (const char*)p.x[br] + ((size_t)(b * 96 + qg * 32 + r) * 448 + 8 * hh) * 2;
    const char* xa = xp + rot * 64;
    const char* xb = xp + rot * 64 - 896;
    #pragma unroll
    for (int s = 0; s < 28; ++s) {
      const char* q = (s + 2 * rot >= 28) ? xb : xa;
      asm volatile("global_load_dwordx4 %0, %1, off offset:%2" : "=v"(xf[s]) : "v"(q), "i"(s * 32) : "memory");
    }
  }
  {
    const u32x4 srdK = make_srd((const char*)p.k[br] + kvoff, 262144u);
    const unsigned hi = lane >> 5, pc = lane & 31;
    const unsigned vb = hi * 512u + ((pc ^ hi) << 4);
    #pragma unroll
    for (int j = 0; j < 32; ++j) {
      const unsigned g = wave + 3 * j;
      dma1<KV_NT>(lds0 + g * 1024u, vb ^ (((2 * g) & 15) << 4), srdK, g * 1024u);
    }
  }
  asm volatile("s_waitcnt vmcnt(32)"
               : "+v"(xf[0]), "+v"(xf[1]), "+v"(xf[2]), "+v"(xf[3]), "+v"(xf[4]), "+v"(xf[5]), "+v"(xf[6]), "+v"(xf[7]), "+v"(xf[8]), "+v"(xf[9]),
                 "+v"(xf[10]), "+v"(xf[11]), "+v"(xf[12]), "+v"(xf[13]), "+v"(xf[14]), "+v"(xf[15]), "+v"(xf[16]), "+v"(xf[17]), "+v"(xf[18]), "+v"(xf[19]),
                 "+v"(xf[20]), "+v"(xf[21]), "+v"(xf[22]), "+v"(xf[23]), "+v"(xf[24]), "+v"(xf[25]), "+v"(xf[26]), "+v"(xf[27]) :: "memory");

  const LAS char* L = (const LAS char*)smem;
  bf16x8 A0[8], A1[8];
  bf16x8 qf[16];
  {
    const unsigned ln = lane_id(), hh = ln >> 5, blk = (ln >> 4) & 1, qq = (ln & 15) >> 2, pp = ln & 3;
    f32x16 qa[8];
    #pragma unroll
    for (int d = 0; d < 8; ++d) qa[d] = f32x16{};
    const unsigned sg = ((pp & 1) << 1) | (pp >> 1);
    unsigned wb[4];
    #pragma unroll
    for (int d = 0; d < 4; ++d) wb[d] = (8 * hh + qq) * 512 + ((d ^ qq) << 6) + 32 * blk + 8 * sg;
    auto rdW = [&](int n, int ks, int dt) { const LAS char* a = L + slot_of(n) * SLOT + wb[dt & 3] + ks * 8192 + (dt >> 2) * 256; return trA(a, a + 2048); };
    bar();
    #pragma unroll
    for (int dt = 0; dt < 8; ++dt) A0[dt] = rdW(0, 0, dt);
    SB();
    #pragma unroll
    for (int n = 0; n < 14; ++n) {
      #pragma unroll
      for (int dt = 0; dt < 8; ++dt) {
        if (dt < 4) { A1[2 * dt] = rdW(n, 1, 2 * dt); A1[2 * dt + 1] = rdW(n, 1, 2 * dt + 1); }
        SB(); qa[dt] = mfma(A0[dt], xf[2 * n], qa[dt]); SB();
      }
      if (n < 13) bar();
      #pragma unroll
      for (int dt = 0; dt < 8; ++dt) {
        if (n < 13 && dt < 4) { A0[2 * dt] = rdW(n + 1, 0, 2 * dt); A0[2 * dt + 1] = rdW(n + 1, 0, 2 * dt + 1); }
        SB(); qa[dt] = mfma(A1[dt], xf[2 * n + 1], qa[dt]); SB();
      }
    }
    #pragma unroll
    for (int d = 0; d < 8; ++d) { qf[2 * d] = pack8n(qa[d], 0); qf[2 * d + 1] = pack8n(qa[d], 1); }
  }
  f32x16 sa[16];
  {
    const unsigned ln = lane_id(), r = ln & 31, hh = ln >> 5;
    const unsigned kb = r * 512 + ((hh ^ (r & 15)) << 4);
    unsigned kbs[8];
    #pragma unroll
    for (int s = 0; s < 8; ++s) kbs[s] = kb ^ (s << 5);
    auto rdK = [&](int i, int half, int s) { return *(const LAS bf16x8*)(L + slot_of(14 + i) * SLOT + kbs[s] + half * 256); };
    waitv<26>(); bar();
    #pragma unroll
    for (int s = 0; s < 8; ++s) A0[s] = rdK(0, 0, s);
    SB();
    #pragma unroll
    for (int i = 0; i < 16; ++i) {
      f32x16 acc = f32x16{};
      #pragma unroll
      for (int s = 0; s < 8; ++s) {
        if (s < 4) { A1[2 * s] = rdK(i, 1, 2 * s); A1[2 * s + 1] = rdK(i, 1, 2 * s + 1); }
        SB(); acc = mfma(A0[s], qf[s], acc); SB();
      }
      if (i == 0) waitv<21>(); else if (i == 1) waitv<16>(); else if (i == 2) waitv<10>(); else if (i == 3) waitv<5>(); else if (i == 4) waitv<0>();
      bar();
      #pragma unroll
      for (int s = 0; s < 8; ++s) {
        if (i < 15 && s < 4) { A0[2 * s] = rdK(i + 1, 0, 2 * s); A0[2 * s + 1] = rdK(i + 1, 0, 2 * s + 1); }
        SB(); acc = mfma(A1[s], qf[8 + s], acc); SB();
      }
      sa[i] = acc;
    }
  }
  bf16x8 pf[32]; float inv_l;
  {
    float m = sa[0][0];
    #pragma unroll
    for (int i = 0; i < 16; ++i)
      #pragma unroll
      for (int e = 0; e < 16; ++e) m = fmaxf(m, sa[i][e]);
    { auto sw = __builtin_amdgcn_permlane32_swap(__float_as_uint(m), __float_as_uint(m), false, false);
      m = fmaxf(__uint_as_float(sw[0]), __uint_as_float(sw[1])); }
    const float c = 0.09016844005556021f;
    const float mc = -m * c;
    float l0 = 0.f, l1 = 0.f;
    #pragma unroll
    for (int i = 0; i < 16; ++i) {
      #pragma unroll
      for (int e = 0; e < 16; e += 2) {
        const float e0 = __builtin_amdgcn_exp2f(__builtin_fmaf(sa[i][e], c, mc)), e1 = __builtin_amdgcn_exp2f(__builtin_fmaf(sa[i][e + 1], c, mc));
        l0 += e0; l1 += e1; sa[i][e] = e0; sa[i][e + 1] = e1;
      }
      pf[2 * i] = pack8n(sa[i], 0); pf[2 * i + 1] = pack8n(sa[i], 1);
      SB();
    }
    float l = l0 + l1;
    { auto sw = __builtin_amdgcn_permlane32_swap(__float_as_uint(l), __float_as_uint(l), false, false);
      l = __uint_as_float(sw[0]) + __uint_as_float(sw[1]); }
    inv_l = __builtin_amdgcn_rcpf(l);
  }
  {
    const unsigned ln = lane_id(), hh = ln >> 5, blk = (ln >> 4) & 1, qq = (ln & 15) >> 2, pp = ln & 3;
    unsigned vb[2];
    #pragma unroll
    for (int dt = 0; dt < 2; ++dt) vb[dt] = (4 * hh + qq) * 128 + ((dt ^ (qq >> 1)) << 6) + 32 * blk + 8 * pp;
    const __amdgpu_buffer_rsrc_t ro = __builtin_amdgcn_make_buffer_rsrc((char*)p.ws + (size_t)(((br * 8 + b) * 16 + h) * 4) * 3 * 4096 + qg * 4096, 0, 4 * 3 * 4096, 0x00020000);
    int slot = 6;
    auto rdV = [&](int sl, int g, int j) { const LAS char* a = L + sl * SLOT + vb[j & 1] + (4 * g + (j >> 1)) * 2048; return trA(a, a + 1024); };
    #pragma unroll
    for (int j = 0; j < 8; ++j) A0[j] = rdV(slot, 0, j);
    SB();
    #pragma unroll 1
    for (int c = 0; c < 4; ++c) {
      f32x16 oa[2] = {f32x16{}, f32x16{}};
      #pragma unroll
      for (int t = 0; t < 4; ++t) {
        #pragma unroll
        for (int j = 0; j < 8; ++j) {
          if (j < 4) { A1[2 * j] = rdV(slot, 1, 2 * j); A1[2 * j + 1] = rdV(slot, 1, 2 * j + 1); }
          SB(); oa[j & 1] = mfma(A0[j], pf[8 * t + (j >> 1)], oa[j & 1]); SB();
        }
        if (t < 3 || c < 3) bar();
        slot = slot == 9 ? 0 : slot + 1;
        #pragma unroll
        for (int j = 0; j < 8; ++j) {
          if (j < 4) { A0[2 * j] = rdV(slot, 0, 2 * j); A0[2 * j + 1] = rdV(slot, 0, 2 * j + 1); }
          SB(); oa[j & 1] = mfma(A1[j], pf[8 * t + 4 + (j >> 1)], oa[j & 1]); SB();
        }
      }
      #pragma unroll
      for (int dt = 0; dt < 2; ++dt)
        #pragma unroll
        for (int ks = 0; ks < 2; ++ks)
          __builtin_amdgcn_raw_buffer_store_b128(__builtin_bit_cast(u32x4, pack8(oa[dt], ks, inv_l)), ro, ln * 16 + c * (3 * 4096) + (dt * 2 + ks) * 1024, 0, 0);
    }
  }
}

__global__ __launch_bounds__(256, 1) void oproj_kernel(Params p) {
  extern __shared__ __attribute__((aligned(16))) char smem[];
  const int u = blockIdx.x;
  const int j = u % 14, b = (u / 14) & 7, br = u / 112;
  const int wave = rfl(threadIdx.x >> 6);
  const unsigned lane = threadIdx.x & 63;
  const unsigned lds0 = (unsigned)(size_t)(LAS char*)smem;
  if (wave == 3) {
    const u32x4 srd = make_srd((const char*)p.wo[br] + j * 64, 4096u * 896u - j * 64u);
    unsigned v[4];
    #pragma unroll
    for (int i = 0; i < 4; ++i) v[i] = (lane >> 2) * 896u + (lane & 3) * 16u + i * (16u * 896u - 1024u);
    auto issue = [&](int it, int slot) {
      const int c = it >> 2, u4 = it & 3;
      #pragma unroll
      for (int g = 0; g < 4; ++g) dma4<0>(lds0 + slot * SLOT + g * 4096, v[0], v[1], v[2], v[3], srd, ((4 * u4 + g) * 256 + 64 * c) * 896u);
    };
    for (int i = 0; i < 3; ++i) issue(i, i);
    #pragma unroll 1
    for (int n = 0; n < 16; ++n) {
      if (n + 3 < 16) { waitv<47>(); issue(n + 3, (n + 3) % 10 ); waitv<48>(); } else if (n == 13) waitv<32>(); else if (n == 14) waitv<16>(); else waitv<0>();
      bar_only();
    }
    return;
  }
  const int qg = wave;
  const unsigned r = lane & 31, hh = lane >> 5;
  const unsigned blk = (lane >> 4) & 1, qq = (lane & 15) >> 2, pp = lane & 3;
  const LAS char* L = (const LAS char*)smem;
  const unsigned ab = (4 * hh + qq) * 64 + 32 * blk + 8 * pp;
  const char* ob = (const char*)p.ws + (size_t)((br * 8 + b) * 16) * 4 * 3 * 4096 + qg * 4096 + lane * 16;
  f32x16 acc = f32x16{};
  int slot = 0;
  #pragma unroll 1
  for (int n = 0; n < 16; ++n) {
    const int c = n >> 2, u4 = n & 3;
    bf16x8 of[16];
    #pragma unroll
    for (int g = 0; g < 4; ++g)
      #pragma unroll
      for (int f = 0; f < 4; ++f) of[4 * g + f] = *(const bf16x8*)(ob + (size_t)((4 * u4 + g) * 4 + c) * (3 * 4096) + f * 1024);
    bar();
    const LAS char* S = L + slot * SLOT;
    slot = slot == 9 ? 0 : slot + 1;
    #pragma unroll
    for (int g = 0; g < 4; ++g)
      #pragma unroll
      for (int f = 0; f < 4; ++f) {
        const LAS char* a = S + ab + (64 * g + 16 * f) * 64;
        acc = mfma(trA(a, a + 512), of[4 * g + f], acc);
      }
  }
  char* op = (char*)p.out + ((size_t)(b * 96 + qg * 32 + r) * 896 + br * 448 + 32 * j + 4 * hh) * 2;
  #pragma unroll
  for (int g = 0; g < 4; ++g) {
    uint2 w; w.x = cvtpk(acc[4 * g], acc[4 * g + 1]); w.y = cvtpk(acc[4 * g + 2], acc[4 * g + 3]);
    *(uint2*)(op + g * 16) = w;
  }
}

extern "C" void kernel_launch(void* const* d_in, const int* in_sizes, int n_in, void* d_out, int out_size, void* d_ws, size_t ws_size, hipStream_t stream) {
  Params p;
  for (int br = 0; br < 2; ++br) { p.x[br] = d_in[br * 5 + 0]; p.k[br] = d_in[br * 5 + 1]; p.v[br] = d_in[br * 5 + 2]; p.wq[br] = d_in[br * 5 + 3]; p.wo[br] = d_in[br * 5 + 4]; }
  p.out = d_out; p.ws = d_ws;
  hipFuncSetAttribute((const void*)attn_kernel, hipFuncAttributeMaxDynamicSharedMemorySize, LDS_BYTES);
  hipFuncSetAttribute((const void*)oproj_kernel, hipFuncAttributeMaxDynamicSharedMemorySize, LDS_BYTES);
  attn_kernel<<<256, 256, LDS_BYTES, stream>>>(p);
  oproj_kernel<<<224, 256, LDS_BYTES, stream>>>(p);
}
```

```cpp
#include <hip/hip_runtime.h>
#include <stdint.h>

#define DI __device__ __forceinline__
#define LAS __attribute__((address_space(3)))
#define GAS __attribute__((address_space(1)))
#define SB() __builtin_amdgcn_sched_barrier(0)
typedef __bf16 bf16x8 __attribute__((ext_vector_type(8)));
typedef __bf16 bf16x4 __attribute__((ext_vector_type(4)));
typedef __bf16 bf16x2 __attribute__((ext_vector_type(2)));
typedef float f32x16 __attribute__((ext_vector_type(16)));
typedef float f32x2 __attribute__((ext_vector_type(2)));
typedef unsigned u32x4 __attribute__((ext_vector_type(4)));
typedef unsigned u32x2 __attribute__((ext_vector_type(2)));

constexpr int KV_NT = 1;
constexpr int W_NT = 0;
constexpr int WINP = 6;
constexpr int CODE_PF = 40;

constexpr int SLOT = 16384, NSLOT = 9;
constexpr int LDS_BYTES = 163840;
constexpr int SPARE = SLOT * NSLOT;
constexpr int CTL = LDS_BYTES - 64;
constexpr unsigned PART_GRP = 28u * 3u * 16u * 1024u;
constexpr unsigned FLAG_OFF = 16u * PART_GRP;
constexpr unsigned EPOCH_OFF = FLAG_OFF + 16u * 84u * 128u;
constexpr unsigned OOB = 0x7ffffff0u;

struct Params {
  const void* x[2]; const void* k[2]; const void* v[2]; const void* wq[2]; const void* wo[2];
  void* out; void* ws;
};

DI unsigned lane_id() { unsigned l; asm volatile("v_mbcnt_lo_u32_b32 %0, -1, 0\n\tv_mbcnt_hi_u32_b32 %0, -1, %0" : "=v"(l)); return l; }
DI unsigned rfl(unsigned v) { return __builtin_amdgcn_readfirstlane(v); }
DI u32x4 make_srd(const void* p, unsigned bytes) {
  unsigned long long a = (unsigned long long)p; u32x4 s;
  s[0] = rfl((unsigned)a); s[1] = rfl((unsigned)(a >> 32)) & 0xffffu; s[2] = bytes; s[3] = 0x00020000u; return s;
}
template <int NT> DI void dma4(unsigned m0v, unsigned v0, unsigned v1, unsigned v2, unsigned v3, u32x4 srd, unsigned soff) {
  if constexpr (NT)
    asm volatile("s_mov_b32 m0, %0\n\ts_nop 0\n\t"
                 "buffer_load_dwordx4 %1, %5, %6 offen nt lds\n\t"
                 "buffer_load_dwordx4 %2, %5, %6 offen offset:1024 nt lds\n\t"
                 "buffer_load_dwordx4 %3, %5, %6 offen offset:2048 nt lds\n\t"
                 "buffer_load_dwordx4 %4, %5, %6 offen offset:3072 nt lds"
                 :: "s"(m0v), "v"(v0), "v"(v1), "v"(v2), "v"(v3), "s"(srd), "s"(soff) : "memory");
  else
    asm volatile("s_mov_b32 m0, %0\n\ts_nop 0\n\t"
                 "buffer_load_dwordx4 %1, %5, %6 offen lds\n\t"
                 "buffer_load_dwordx4 %2, %5, %6 offen offset:1024 lds\n\t"
                 "buffer_load_dwordx4 %3, %5, %6 offen offset:2048 lds\n\t"
                 "buffer_load_dwordx4 %4, %5, %6 offen offset:3072 lds"
                 :: "s"(m0v), "v"(v0), "v"(v1), "v"(v2), "v"(v3), "s"(srd), "s"(soff) : "memory");
}
template <int N> DI void waitv() { asm volatile("s_waitcnt vmcnt(%0)" :: "i"(N) : "memory"); }
DI void bar() { asm volatile("s_waitcnt lgkmcnt(0)\n\ts_barrier" ::: "memory"); }
DI void bar_only() { asm volatile("s_barrier" ::: "memory"); }
DI bf16x8 trA(const LAS char* p0, const LAS char* p1) {
  bf16x4 a = __builtin_amdgcn_ds_read_tr16_b64_v4bf16((LAS bf16x4*)p0);
  bf16x4 b = __builtin_amdgcn_ds_read_tr16_b64_v4bf16((LAS bf16x4*)p1);
  return __builtin_shufflevector(a, b, 0, 1, 2, 3, 4, 5, 6, 7);
}
DI f32x16 mfma(bf16x8 a, bf16x8 b, f32x16 c) { return __builtin_amdgcn_mfma_f32_32x32x16_bf16(a, b, c, 0, 0, 0); }
DI void mfma_v(f32x16& c, bf16x8 a, bf16x8 b) { asm volatile("v_mfma_f32_32x32x16_bf16 %0, %1, %2, %0" : "+v"(c) : "v"(a), "v"(b)); }
DI void mfma_v0(f32x16& c, bf16x8 a, bf16x8 b) { asm volatile("v_mfma_f32_32x32x16_bf16 %0, %1, %2, 0" : "=&v"(c) : "v"(a), "v"(b)); }
DI void mfma_s(f32x16& c, bf16x8 a, bf16x8 b) { asm volatile("s_nop 1\n\tv_mfma_f32_32x32x16_bf16 %0, %1, %2, %0" : "+v"(c) : "v"(a), "v"(b)); }
DI void mfma_s0(f32x16& c, bf16x8 a, bf16x8 b) { asm volatile("s_nop 1\n\tv_mfma_f32_32x32x16_bf16 %0, %1, %2, 0" : "=&v"(c) : "v"(a), "v"(b)); }
DI void mfma_zero_a(f32x16& c, bf16x8 z) { asm volatile("s_nop 1\n\tv_mfma_f32_32x32x16_bf16 %0, %1, %1, 0" : "=a"(c) : "v"(z)); }
DI void mfma_pad(f32x16& a, f32x16& b) { asm volatile("s_nop 7\n\ts_nop 3" : "+v"(a), "+v"(b)); }
DI void mfma_pad(f32x16& a) { asm volatile("s_nop 7\n\ts_nop 3" : "+v"(a)); }
DI unsigned cvtpk(float lo, float hi) { f32x2 v = {lo, hi}; bf16x2 b = __builtin_convertvector(v, bf16x2); return __builtin_bit_cast(unsigned, b); }
DI bf16x8 pack8(const f32x16& a, int s, float sc) {
  u32x4 w;
  w[0] = cvtpk(a[8 * s + 0] * sc, a[8 * s + 1] * sc); w[1] = cvtpk(a[8 * s + 2] * sc, a[8 * s + 3] * sc);
  w[2] = cvtpk(a[8 * s + 4] * sc, a[8 * s + 5] * sc); w[3] = cvtpk(a[8 * s + 6] * sc, a[8 * s + 7] * sc);
  return __builtin_bit_cast(bf16x8, w);
}
DI bf16x8 pack8n(const f32x16& a, int s) {
  u32x4 w;
  w[0] = cvtpk(a[8 * s + 0], a[8 * s + 1]); w[1] = cvtpk(a[8 * s + 2], a[8 * s + 3]);
  w[2] = cvtpk(a[8 * s + 4], a[8 * s + 5]); w[3] = cvtpk(a[8 * s + 6], a[8 * s + 7]);
  return __builtin_bit_cast(bf16x8, w);
}
DI float xhalf_max(float m) { auto sw = __builtin_amdgcn_permlane32_swap(__float_as_uint(m), __float_as_uint(m), false, false); return fmaxf(__uint_as_float(sw[0]), __uint_as_float(sw[1])); }
DI float xhalf_sum(float l) { auto sw = __builtin_amdgcn_permlane32_swap(__float_as_uint(l), __float_as_uint(l), false, false); return __uint_as_float(sw[0]) + __uint_as_float(sw[1]); }
#define XP8(MASK, PERM, D0, D1, K0, K1, S0, S1)                                                                                     \
  asm volatile("s_mov_b32 vcc_lo, " MASK "\n\ts_mov_b32 vcc_hi, " MASK "\n\t"                                                      \
      "v_cndmask_b32_dpp %0, %16, %8, vcc " PERM " row_mask:0xf bank_mask:0xf\n\t"                                                  \
      "v_cndmask_b32_dpp %1, %17, %9, vcc " PERM " row_mask:0xf bank_mask:0xf\n\t"                                                  \
      "v_cndmask_b32_dpp %2, %18, %10, vcc " PERM " row_mask:0xf bank_mask:0xf\n\t"                                                 \
      "v_cndmask_b32_dpp %3, %19, %11, vcc " PERM " row_mask:0xf bank_mask:0xf\n\t"                                                 \
      "v_cndmask_b32_dpp %4, %20, %12, vcc " PERM " row_mask:0xf bank_mask:0xf\n\t"                                                 \
      "v_cndmask_b32_dpp %5, %21, %13, vcc " PERM " row_mask:0xf bank_mask:0xf\n\t"                                                 \
      "v_cndmask_b32_dpp %6, %22, %14, vcc " PERM " row_mask:0xf bank_mask:0xf\n\t"                                                 \
      "v_cndmask_b32_dpp %7, %23, %15, vcc " PERM " row_mask:0xf bank_mask:0xf"                                                      \
      : "=&v"(D0[0]), "=&v"(D0[1]), "=&v"(D0[2]), "=&v"(D0[3]), "=&v"(D1[0]), "=&v"(D1[1]), "=&v"(D1[2]), "=&v"(D1[3])               \
      : "v"(K0[0]), "v"(K0[1]), "v"(K0[2]), "v"(K0[3]), "v"(K1[0]), "v"(K1[1]), "v"(K1[2]), "v"(K1[3]),                               \
        "v"(S0[0]), "v"(S0[1]), "v"(S0[2]), "v"(S0[3]), "v"(S1[0]), "v"(S1[1]), "v"(S1[2]), "v"(S1[3]) : "vcc")
DI void quad_transpose(u32x4& r0, u32x4& r1, u32x4& r2, u32x4& r3) {
  unsigned t0[4], t1[4], t2[4], t3[4], f0[4], f1[4], f2[4], f3[4];
  unsigned a0[4] = {r0[0], r0[1], r0[2], r0[3]}, a1[4] = {r1[0], r1[1], r1[2], r1[3]}, a2[4] = {r2[0], r2[1], r2[2], r2[3]}, a3[4] = {r3[0], r3[1], r3[2], r3[3]};
  XP8("0x55555555", "quad_perm:[1,0,3,2]", t0, t2, a0, a2, a1, a3);
  XP8("0xaaaaaaaa", "quad_perm:[1,0,3,2]", t1, t3, a1, a3, a0, a2);
  XP8("0x33333333", "quad_perm:[2,3,0,1]", f0, f1, t0, t1, t2, t3);
  XP8("0xcccccccc", "quad_perm:[2,3,0,1]", f2, f3, t2, t3, t0, t1);
  r0 = u32x4{f0[0], f0[1], f0[2], f0[3]}; r1 = u32x4{f1[0], f1[1], f1[2], f1[3]}; r2 = u32x4{f2[0], f2[1], f2[2], f2[3]}; r3 = u32x4{f3[0], f3[1], f3[2], f3[3]};
}
DI int nxt(int s) { return s == NSLOT - 1 ? 0 : s + 1; }

#define GROUP(N, NR, CUR, NXT, RD, MM)                                             \
  _Pragma("unroll") for (int j_ = 0; j_ < (N); ++j_) {                             \
    if (j_ < (NR)) NXT[j_] = RD(j_);                                               \
    SB(); MM(j_, CUR[j_]); SB();                                                   \
  }
#define STEP2(NA, NB, NM, NN, PA, PB, RDB, MMA, RDA, MMB)                          \
  _Pragma("unroll") for (int j_ = 0; j_ < (NA); ++j_) {                            \
    if (j_ < (NB)) PB[j_] = RDB(j_);                                               \
    if (j_ == 1) peek();                                                           \
    if (j_ == (NA) - 3) prep();                                                    \
    SB(); MMA(j_, PA[j_]); SB();                                                   \
  }                                                                                \
  move();                                                                          \
  _Pragma("unroll") for (int j_ = 0; j_ < ((NM) > (NN) ? (NM) : (NN)); ++j_) {     \
    if (j_ < (NN)) PA[j_] = RDA(j_);                                               \
    SB(); if (j_ < (NM)) MMB(j_, PB[j_]); SB();                                    \
  }                                                                                \
  release();
#define STEP1(NN, CUR, NXT, RD, MM, FILL)                                          \
  move();                                                                          \
  _Pragma("unroll") for (int j_ = 0; j_ < 8; ++j_) {                               \
    if (j_ < (NN)) NXT[j_] = RD(j_);                                               \
    if (j_ == 1) peek();                                                           \
    if (j_ == 5) prep();                                                           \
    SB(); MM(j_, CUR[j_]); SB();                                                   \
    FILL(j_); SB();                                                                \
  }                                                                                \
  release();

__global__ __launch_bounds__(256, 1) void fused_kernel(Params p) {
  extern __shared__ __attribute__((aligned(16))) char smem[];
  unsigned long long pc0; asm volatile("s_getpc_b64 %0" : "=s"(pc0));
  const int u = blockIdx.x;
  const int xcd = u & 7, idx = u >> 3;
  const int br = xcd >> 2, h = 4 * (xcd & 3) + (idx & 3), b = idx >> 2;
  const int wave = rfl(threadIdx.x >> 6);
  if (wave == 3) {
    const char* code = (const char*)(pc0 & ~255ull) + lane_id() * 16;
    const unsigned dst = (unsigned)(size_t)(LAS char*)smem + SPARE;
    asm volatile("s_mov_b32 m0, %0\n\ts_nop 0\n\tglobal_load_lds_dwordx4 %1, off" :: "s"(dst), "v"(code + min(idx, CODE_PF - 1) * 1024) : "memory");
    asm volatile("s_mov_b32 m0, %0\n\ts_nop 0\n\tglobal_load_lds_dwordx4 %1, off" :: "s"(dst), "v"(code + min(idx + 32, CODE_PF - 1) * 1024) : "memory");
  }
  const char* const px = (const char*)(br ? p.x[1] : p.x[0]);
  const char* const pk = (const char*)(br ? p.k[1] : p.k[0]);
  const char* const pv = (const char*)(br ? p.v[1] : p.v[0]);
  const char* const pwq = (const char*)(br ? p.wq[1] : p.wq[0]);
  const char* const pwo = (const char*)(br ? p.wo[1] : p.wo[0]);
  char* const pws = (char*)p.ws;
  char* const pout = (char*)p.out;
  const unsigned lds0 = (unsigned)(size_t)(LAS char*)smem;
  const size_t kvoff = (size_t)(b * 16 + h) * (512 * 256 * 2);
  const int rot = (b * 7) >> 3;

  if (wave == 3) {
    const unsigned lane = lane_id();
    if (lane < 16) ((volatile LAS unsigned*)(smem + CTL))[lane] = 0u;
    const u32x4 srdW = make_srd(pwq + h * 512, 448u * 8192u - h * 512u);
    const u32x4 srdK = make_srd(pk + kvoff, 262144u);
    const u32x4 srdV = make_srd(pv + kvoff, 262144u);
    const u32x4 srdO = make_srd(pwo + (size_t)h * 256 * 896, 256u * 896u);
    unsigned vW[4], vK[4], vV[4], vO[4];
    { const unsigned r8 = lane >> 3, c8 = lane & 7, sw1 = (c8 ^ (((r8 >> 1) & 1) << 2)) << 4;
      #pragma unroll
      for (int i = 0; i < 4; ++i) {
        vW[i] = r8 * 8192u + sw1 + i * (8u * 8192u - 1024u);
        vK[i] = r8 * 512u + ((c8 ^ ((4 * (i & 1) + (r8 >> 1)) & 7)) << 4) + i * 3072u;
      } }
    auto late_patterns = [&]() {
      const unsigned l2 = lane_id(), r = l2 >> 3, sw = ((l2 & 7) ^ (((r >> 1) & 1) << 2)) << 4;
      #pragma unroll
      for (int i = 0; i < 4; ++i) { vV[i] = r * 512u + sw + i * 3072u; vO[i] = r * 896u + sw + i * (8u * 896u - 1024u); }
    };
    const unsigned vD = OOB;
    auto partW = [&](unsigned m0, int c, int j) {
      int jj = j + rot; if (jj >= 7) jj -= 7;
      const unsigned so = jj * (64u * 8192u) + c * 128u;
      dma4<W_NT>(m0, vW[0], vW[1], vW[2], vW[3], srdW, so); dma4<W_NT>(m0 + 4096, vW[0], vW[1], vW[2], vW[3], srdW, so + 32u * 8192u);
    };
    auto partK = [&](unsigned m0, int c, int j) {
      const unsigned so = j * 32768u + c * 128u;
      dma4<KV_NT>(m0, vK[0], vK[1], vK[2], vK[3], srdK, so); dma4<KV_NT>(m0 + 4096, vK[0], vK[1], vK[2], vK[3], srdK, so + 16384u);
    };
    auto partV = [&](unsigned m0, int c, int j) {
      const unsigned so = j * 32768u + c * 128u;
      dma4<KV_NT>(m0, vV[0], vV[1], vV[2], vV[3], srdV, so); dma4<KV_NT>(m0 + 4096, vV[0], vV[1], vV[2], vV[3], srdV, so + 16384u);
    };
    auto partO = [&](unsigned m0, int c, int j) {
      const unsigned so = c * (64u * 896u) + j * 128u;
      dma4<W_NT>(m0, vO[0], vO[1], vO[2], vO[3], srdO, so); dma4<W_NT>(m0 + 4096, vO[0], vO[1], vO[2], vO[3], srdO, so + 32u * 896u);
    };
    auto partD = [&](unsigned m0) { dma4<0>(m0, vD, vD, vD, vD, srdO, 0); dma4<0>(m0 + 4096, vD, vD, vD, vD, srdO, 0); };
    auto sl = [&](int s) { return lds0 + s * SLOT; };
    volatile LAS unsigned* landed = (volatile LAS unsigned*)(smem + CTL);
    volatile LAS u32x4* cons = (volatile LAS u32x4*)(smem + CTL + 16);
    int P = 0, pub = 0;
    auto publish = [&](int v) { pub = max(pub, v); *landed = pub; };
    auto gate = [&]() { waitv<8 * (WINP - 1)>(); publish(P - (WINP - 1)); };
    auto drain = [&]() {
      if constexpr (WINP > 6) { waitv<48>(); publish(P - 6); }
      if constexpr (WINP > 5) { waitv<40>(); publish(P - 5); }
      if constexpr (WINP > 4) { waitv<32>(); publish(P - 4); }
      waitv<24>(); publish(P - 3); waitv<16>(); publish(P - 2); waitv<8>(); publish(P - 1); waitv<0>(); publish(P);
    };
    auto freed = [&](int need) { const u32x4 c = *cons; const int m = min(min((int)rfl(c[0]), (int)rfl(c[1])), (int)rfl(c[2])); return m >= need; };
    auto slot_wait = [&](int step) {
      const int need = step - NSLOT + 1;
      if (!freed(need)) { drain(); for (unsigned sp = 0; sp < (1u << 22) && !freed(need); ++sp) __builtin_amdgcn_s_sleep(1); }
    };
    bar();
    #pragma unroll
    for (int j = 0; j < 7; ++j) { partW(sl(j >> 1) + (j & 1) * 8192, 0, j); ++P; }
    late_patterns();
    int slot = 4;
    #pragma unroll 1
    for (int m = 4; m < 36; ++m) {
      const int s = m - 4, c = s >> 3, j = s & 7;
      if (m >= NSLOT) slot_wait(m);
      gate(); partK(sl(slot), c, j); ++P;
      gate();
      if (c < 3 && j < 7) partW(sl(slot) + 8192, c + 1, j); else partD(sl(slot) + 8192); ++P;
      slot = nxt(slot);
    }
    #pragma unroll 1
    for (int m = 36; m < 68; ++m) {
      const int s = m - 36, c = s >> 3, j = s & 7;
      slot_wait(m);
      gate(); partV(sl(slot), c, j); ++P;
      gate(); if (c > 0 && j < 7) partO(sl(slot) + 8192, c - 1, j); else partD(sl(slot) + 8192); ++P;
      slot = nxt(slot);
    }
    #pragma unroll 1
    for (int m = 68; m < 72; ++m) {
      const int q = m - 68;
      slot_wait(m);
      gate(); partO(sl(slot), 3, 2 * q); ++P;
      gate(); if (q < 3) partO(sl(slot) + 8192, 3, 2 * q + 1); else partD(sl(slot) + 8192); ++P;
      slot = nxt(slot);
    }
    drain();
    return;
  }

  const int qg = wave;
  const int grp = br * 8 + b;
  f32x16 sa[16];
  u32x4 xr[28];
  unsigned long long epoch;
  {
    const unsigned lane = lane_id();
    asm volatile("global_load_dwordx2 %0, %1, off sc1" : "=v"(epoch) : "v"(pws + EPOCH_OFF + grp * 128) : "memory");
    const char* xp = px + (size_t)(b * 96 + qg * 32 + 4 * ((lane & 31) >> 2)) * 896 + (4 * (lane >> 5) + (lane & 3)) * 16;
    #pragma unroll
    for (int a = 0; a < 7; ++a) {
      int aa = a + rot; if (aa >= 7) aa -= 7;
      const char* q = xp + aa * 128;
      asm volatile("global_load_dwordx4 %0, %1, off" : "=v"(xr[4 * a + 0]) : "v"(q) : "memory");
      asm volatile("global_load_dwordx4 %0, %1, off offset:896" : "=v"(xr[4 * a + 1]) : "v"(q) : "memory");
      asm volatile("global_load_dwordx4 %0, %1, off offset:1792" : "=v"(xr[4 * a + 2]) : "v"(q) : "memory");
      asm volatile("global_load_dwordx4 %0, %1, off offset:2688" : "=v"(xr[4 * a + 3]) : "v"(q) : "memory");
    }
  }
  bar_only();
  { bf16x8 z = {}; asm volatile("" : "+v"(z));
    #pragma unroll
    for (int i = 0; i < 16; ++i) mfma_zero_a(sa[i], z); }
#define XBLOCK(A) do {                                                                                                              \
    asm volatile("s_waitcnt vmcnt(%4)" : "+v"(xr[4 * (A)]), "+v"(xr[4 * (A) + 1]), "+v"(xr[4 * (A) + 2]), "+v"(xr[4 * (A) + 3]) : "i"(4 * (6 - (A))) : "memory"); \
    quad_transpose(xr[4 * (A)], xr[4 * (A) + 1], xr[4 * (A) + 2], xr[4 * (A) + 3]); } while (0)
  asm volatile("s_waitcnt vmcnt(%1)" : "+v"(epoch) : "i"(28) : "memory");
  XBLOCK(0); XBLOCK(1);
  const unsigned long long tag = ((unsigned long long)rfl((unsigned)(epoch >> 32)) << 32 | rfl((unsigned)epoch)) * 0x9E3779B97F4A7C15ull + 0xD1B54A32D192ED03ull;
  const LAS char* L = (const LAS char*)smem;
  auto opaque = [&](unsigned a) { unsigned r; asm volatile("v_mov_b32 %0, %1" : "=v"(r) : "s"(a)); return r; };
  volatile LAS unsigned* landed = (volatile LAS unsigned*)(size_t)opaque(lds0 + CTL);
  volatile LAS unsigned* mycons = (volatile LAS unsigned*)(size_t)opaque(lds0 + CTL + 16 + 4 * wave);
  int step = 0, slot = 0;
  const LAS char* SA = L;
  unsigned seen = 0;
  auto peek = [&]() { seen = *landed; };
  auto spin = [&](int need) { for (unsigned sp = 0; sp < (1u << 22); ++sp) { if ((int)rfl(*landed) >= need) break; } asm volatile("" ::: "memory"); };
  int extra = 0;
  int need = 0, nslot = 0; bool ready = false;
  auto prep = [&]() {
    need = extra ? extra : 2 * (step + 1) + 1;
    ready = (int)rfl(seen) >= need;
    nslot = nxt(slot);
  };
  auto move = [&]() {
    if (__builtin_expect(!ready, 0)) {
      spin(need);
    }
    ++step; slot = nslot; SA = L + slot * SLOT;
  };
  auto release = [&]() { asm volatile("" ::: "memory"); *mycons = step; };
  auto nofill = [&](int) {};
  bf16x8 A0[8], A1[8];
  bf16x8 pf[32];
  float inv_l;
  {
    const unsigned ln = lane_id(), r = ln & 31, hh = ln >> 5, blk = (ln >> 4) & 1, qq = (ln & 15) >> 2, pp = ln & 3;
    const unsigned sg = ((pp & 1) << 1) | (pp >> 1);
    unsigned wb[2], kbs[4];
    #pragma unroll
    for (int d = 0; d < 2; ++d) wb[d] = (32 * hh + qq) * 128 + ((d ^ (qq >> 1)) << 6) + 32 * blk + 8 * sg;
    #pragma unroll
    for (int s = 0; s < 4; ++s) kbs[s] = (r * 128 + ((hh ^ ((r >> 1) & 7)) << 4)) ^ (s << 5);
    f32x16 qa[2];
    bf16x8 qf[4];
    const float cs = 0.09016844005556021f;
    auto rdWa = [&](int i) { const LAS char* a = SA + wb[i & 1] + (i >> 1) * 1024; return trA(a, a + 512); };
    auto rdWb = [&](int i) { const LAS char* a = SA + 8192 + wb[i & 1] + (i >> 1) * 1024; return trA(a, a + 512); };
    auto rdK = [&](int i) { return *(const LAS bf16x8*)(SA + kbs[i >> 1] + (i & 1) * 4096); };
#define MMQ(J) [&](int i, bf16x8 a) { if ((J) == 0 && i < 2) mfma_v0(qa[i & 1], a, __builtin_bit_cast(bf16x8, xr[i >> 1])); else mfma_v(qa[i & 1], a, __builtin_bit_cast(bf16x8, xr[4 * (J) + (i >> 1)])); }
    spin(2);
    #pragma unroll
    for (int i = 0; i < 8; ++i) A0[i] = rdWa(i);
    SB();
    extra = 4; STEP2(8, 8, 8, 8, A0, A1, rdWb, MMQ(0), rdWa, MMQ(1))
    XBLOCK(2); XBLOCK(3);
    extra = 6; STEP2(8, 8, 8, 8, A0, A1, rdWb, MMQ(2), rdWa, MMQ(3))
    XBLOCK(4); XBLOCK(5);
    extra = 7; STEP2(8, 8, 8, 8, A0, A1, rdWb, MMQ(4), rdWa, MMQ(5))
    XBLOCK(6);
    extra = 0;
    {
      waitv<0>();
      *(volatile LAS unsigned*)(smem + CTL + 32 + 4 * wave) = 1u;
      volatile LAS u32x4* kok = (volatile LAS u32x4*)(smem + CTL + 32);
      for (unsigned sp = 0; sp < (1u << 22); ++sp) { const u32x4 c = *kok; if (rfl(c[0]) & rfl(c[1]) & rfl(c[2])) break; }
    }
    peek(); prep();
    STEP1(8, A0, A1, rdK, MMQ(6), nofill)
    mfma_pad(qa[0], qa[1]);
    #pragma unroll
    for (int i = 0; i < 4; ++i) qf[i] = pack8(qa[i >> 1], i & 1, cs);
    #pragma unroll 1
    for (int c = 0; c < 3; ++c) {
      #pragma unroll
      for (int j = 0; j < 8; ++j) {
        auto mmS = [&](int i, bf16x8 a) { sa[2 * j + (i & 1)] = mfma(a, qf[i >> 1], sa[2 * j + (i & 1)]); };
        if (j < 7) { STEP2(8, 8, 8, 8, A1, A0, rdWb, mmS, rdK, MMQ(j)) }
        else {
          STEP2(8, 0, 0, 8, A1, A0, rdWb, mmS, rdK, mmS)
          mfma_pad(qa[0], qa[1]);
          #pragma unroll
          for (int i = 0; i < 4; ++i) qf[i] = pack8(qa[i >> 1], i & 1, cs);
        }
      }
    }
    bf16x8 ones, negm = {};
    #pragma unroll
    for (int j = 0; j < 8; ++j) ones[j] = (__bf16)1.0f;
    asm volatile("" : "+v"(ones));
    auto shift = [&](int t) { sa[t] = mfma(ones, negm, sa[t]); };
    auto soft = [&](int t, int part, float sub, bool dosub) __attribute__((always_inline)) {
      const int tt = t + (part >> 2), e = 4 * (part & 3);
      if (e == 0) asm volatile("" : "+a"(sa[tt]));
      float s0 = sa[tt][e], s1 = sa[tt][e + 1], s2 = sa[tt][e + 2], s3 = sa[tt][e + 3];
      if (dosub) { s0 -= sub; s1 -= sub; s2 -= sub; s3 -= sub; }
      unsigned w0 = cvtpk(__builtin_amdgcn_exp2f(s0), __builtin_amdgcn_exp2f(s1)), w1 = cvtpk(__builtin_amdgcn_exp2f(s2), __builtin_amdgcn_exp2f(s3));
      asm volatile("" : "+v"(w0), "+v"(w1));
      u32x4 w = __builtin_bit_cast(u32x4, pf[2 * tt + (e >> 3)]);
      w[(e & 7) >> 1] = w0; w[((e & 7) >> 1) + 1] = w1;
      pf[2 * tt + (e >> 3)] = __builtin_bit_cast(bf16x8, w);
    };
    auto softm = [&](int t, int part, auto&& mid) __attribute__((always_inline)) {
      const int tt = t + (part >> 2), e = 4 * (part & 3);
      if (e == 0) asm volatile("" : "+a"(sa[tt]));
      float p0 = __builtin_amdgcn_exp2f(sa[tt][e]), p1 = __builtin_amdgcn_exp2f(sa[tt][e + 1]);
      float s2 = sa[tt][e + 2], s3 = sa[tt][e + 3];
      asm volatile("" : "+v"(p0), "+v"(p1), "+v"(s2), "+v"(s3));
      SB(); mid(); SB();
      unsigned w0 = cvtpk(p0, p1), w1 = cvtpk(__builtin_amdgcn_exp2f(s2), __builtin_amdgcn_exp2f(s3));
      asm volatile("" : "+v"(w0), "+v"(w1));
      u32x4 w = __builtin_bit_cast(u32x4, pf[2 * tt + (e >> 3)]);
      w[(e & 7) >> 1] = w0; w[((e & 7) >> 1) + 1] = w1;
      pf[2 * tt + (e >> 3)] = __builtin_bit_cast(bf16x8, w);
    };
    f32x16 la;
    auto sum1 = [&](int t, int k) __attribute__((always_inline)) {
      if (t == 0 && k == 0) mfma_s0(la, ones, pf[0]); else mfma_s(la, ones, pf[2 * t + k]);
    };
    auto sum4 = [&](int t) __attribute__((always_inline)) { sum1(t, 0); sum1(t, 1); sum1(t, 2); sum1(t, 3); };
    peek(); prep();
    #pragma unroll
    for (int j = 0; j < 8; j += 2) {
      auto mmS0 = [&](int i, bf16x8 a) { sa[2 * j + (i & 1)] = mfma(a, qf[i >> 1], sa[2 * j + (i & 1)]); };
      auto mmS1 = [&](int i, bf16x8 a) { sa[2 * j + 2 + (i & 1)] = mfma(a, qf[i >> 1], sa[2 * j + 2 + (i & 1)]); };
      auto f0 = [&](int g) { if (j > 0) softm(2 * j - 2, g, [&]() { if (g >= 6) shift(2 * j + (g & 1)); else if (j >= 2 && g >= 1 && g < 5) sum1(2 * j - 4, g - 1); }); };
      auto f1 = [&](int g) { softm(2 * j, g, [&]() { if (g >= 6) shift(2 * j + 2 + (g & 1)); else if (j > 0 && g >= 1 && g < 5) sum1(2 * j - 2, g - 1); }); };
      STEP1(8, A1, A0, rdK, mmS0, f0)
      if (j == 0) {
        float m = sa[0][0];
        #pragma unroll
        for (int e = 1; e < 16; ++e) m = fmaxf(m, sa[0][e]);
        #pragma unroll
        for (int e = 0; e < 16; ++e) m = fmaxf(m, sa[1][e]);
        m = xhalf_max(m);
        u32x4 w = {hh == 0 ? cvtpk(-m, 0.f) : 0u, 0u, 0u, 0u};
        asm volatile("" : "+v"(w), "+a"(sa[0]), "+a"(sa[1]));
        negm = __builtin_bit_cast(bf16x8, w);
        shift(0); shift(1);
      }
      if (j < 6) { STEP1(8, A0, A1, rdK, mmS1, f1) } else { STEP1(0, A0, A1, rdK, mmS1, f1) }
    }
    #pragma unroll
    for (int g = 0; g < 8; ++g) soft(14, g, 0.f, false);
    sum4(12); sum4(14);
    mfma_pad(la);
    float l = la[0];
    if (__builtin_expect(__any(!(l < 16777216.f && l > 5.9604645e-8f)), 0)) {
      #pragma unroll
      for (int i = 0; i < 16; ++i) asm volatile("" : "+a"(sa[i]));
      float m = -__builtin_inff();
      #pragma unroll
      for (int i = 0; i < 16; ++i) {
        #pragma unroll
        for (int e = 0; e < 16; ++e) m = fmaxf(m, sa[i][e]);
        SB();
      }
      #pragma unroll
      for (int i = 0; i < 16; ++i) asm volatile("" : "+a"(sa[i]));
      m = xhalf_max(m);
      #pragma unroll
      for (int t = 0; t < 16; t += 2)
        #pragma unroll
        for (int g = 0; g < 8; ++g) { soft(t, g, m, true); SB(); }
      #pragma unroll
      for (int t = 0; t < 16; t += 2) sum4(t);
      mfma_pad(la);
      l = la[0];
    }
    inv_l = __builtin_amdgcn_rcpf(l);
  }
  f32x16 oa[14];
  landed = (volatile LAS unsigned*)(size_t)opaque(lds0 + CTL);
  mycons = (volatile LAS unsigned*)(size_t)opaque(lds0 + CTL + 16 + 4 * wave);
  {
    const unsigned ln = lane_id(), hh = ln >> 5, blk = (ln >> 4) & 1, qq = (ln & 15) >> 2, pp = ln & 3;
    unsigned vb[2];
    #pragma unroll
    for (int dt = 0; dt < 2; ++dt) vb[dt] = (4 * hh + qq) * 128 + ((dt ^ (qq >> 1)) << 6) + 32 * blk + 8 * pp;
    auto rdV = [&](int i) { const LAS char* a = SA + vb[i & 1] + (i >> 1) * 2048; return trA(a, a + 1024); };
    auto rdOb = [&](int i) { const LAS char* a = SA + 8192 + vb[i & 1] + (i >> 1) * 2048; return trA(a, a + 1024); };
    f32x16 va[2] = {f32x16{}, f32x16{}};
    bf16x8 of[4];
    #pragma unroll
    for (int i = 0; i < 8; ++i) A1[i] = rdV(i);
    SB();
    peek(); prep();
    #pragma unroll
    for (int j = 0; j < 8; j += 2) {
      auto mm0 = [&](int i, bf16x8 a) { va[i & 1] = mfma(a, pf[4 * j + (i >> 1)], va[i & 1]); };
      auto mm1 = [&](int i, bf16x8 a) { va[i & 1] = mfma(a, pf[4 * j + 4 + (i >> 1)], va[i & 1]); };
      auto zq = [&](int n) __attribute__((always_inline)) {
        if (n < 56) {
          #pragma unroll
          for (int e = 0; e < 4; ++e) { float x; asm volatile("v_accvgpr_write_b32 %0, 0" : "=a"(x)); oa[n >> 2][4 * (n & 3) + e] = x; }
        }
      };
      auto z0 = [&](int g) { zq(8 * j + g); };
      auto z1 = [&](int g) { zq(8 * j + 8 + g); };
      STEP1(8, A1, A0, rdV, mm0, z0)
      STEP1(8, A0, A1, rdV, mm1, z1)
    }
    #pragma unroll
    for (int i = 0; i < 4; ++i) of[i] = pack8(va[i >> 1], i & 1, inv_l);
    va[0] = f32x16{}; va[1] = f32x16{};
    #pragma unroll 1
    for (int c = 1; c < 4; ++c) {
      #pragma unroll
      for (int j = 0; j < 8; ++j) {
        auto mmV = [&](int i, bf16x8 a) { va[i & 1] = mfma(a, pf[4 * j + (i >> 1)], va[i & 1]); };
        auto mmO = [&](int i, bf16x8 a) { oa[2 * j + (i & 1)] = mfma(a, of[i >> 1], oa[2 * j + (i & 1)]); };
        if (j < 7) { STEP2(8, 8, 8, 8, A1, A0, rdOb, mmV, rdV, mmO) }
        else { STEP2(8, 0, 0, 8, A1, A0, rdOb, mmV, rdV, mmV) }
      }
      #pragma unroll
      for (int i = 0; i < 4; ++i) of[i] = pack8(va[i >> 1], i & 1, inv_l);
      va[0] = f32x16{}; va[1] = f32x16{};
    }
    const __amdgpu_buffer_rsrc_t rp = __builtin_amdgcn_make_buffer_rsrc(pws + (size_t)grp * PART_GRP, 0, PART_GRP, 0x00020000);
    const unsigned vo = (qg * 16 + h) * 1024 + ln * 16;
    GAS unsigned long long* fl = (GAS unsigned long long*)(pws + FLAG_OFF) + grp * (84 * 16);
    auto put = [&](int t, int f) __attribute__((always_inline)) {
      if (f == 0) asm volatile("" : "+a"(oa[t]));
      __builtin_amdgcn_raw_buffer_store_b128(__builtin_bit_cast(u32x4, pack8n(oa[t], f)), rp, vo + (2 * t + f) * (3 * 16 * 1024), 0, 16);
      asm volatile("" ::: "memory");
    };
    auto flag = [&](int j) __attribute__((always_inline)) {
      if (ln < 4) __hip_atomic_store(fl + ((4 * j + ln) * 3 + qg) * 16 + h, tag, __ATOMIC_RELAXED, __HIP_MEMORY_SCOPE_AGENT);
      asm volatile("" ::: "memory");
    };
    #pragma unroll
    for (int q = 0; q < 4; ++q) {
      auto mmA = [&](int i, bf16x8 a) { oa[4 * q + (i & 1)] = mfma(a, of[i >> 1], oa[4 * q + (i & 1)]); if (q > 0 && (i & 1)) put(4 * q - 2 + (i >> 2), (i >> 1) & 1); };
      auto mmB = [&](int i, bf16x8 a) {
        if (i == 0 && q == 2) { waitv<9>(); flag(1); }
        if (i == 0 && q == 3) {}
        oa[4 * q + 2 + (i & 1)] = mfma(a, of[i >> 1], oa[4 * q + 2 + (i & 1)]); if (i & 1) put(4 * q + (i >> 2), (i >> 1) & 1); };
      if (q < 3) {
        STEP2(8, 8, 8, 8, A1, A0, rdOb, mmA, rdV, mmB)
        if (q == 1) { waitv<8>(); flag(0); }
        if (q == 2) { waitv<10>(); flag(2); }
      } else { GROUP(8, 0, A1, A0, rdOb, mmA) waitv<10>(); flag(3); }
    }
    put(12, 0); put(12, 1); put(13, 0); put(13, 1);
    {
      const int idx2 = qg * 16 + h;
      const int pA = h, gA = qg, pB = 16 + idx2 / 3, gB = idx2 % 3;
      auto look = [&](int p, int g) __attribute__((always_inline)) { return __hip_atomic_load(fl + (p * 3 + g) * 16 + (ln & 15), __ATOMIC_RELAXED, __HIP_MEMORY_SCOPE_AGENT); };
      waitv<10>(); flag(4); waitv<6>(); flag(5); waitv<2>(); flag(6);
      unsigned long long fA = look(pA, gA);
      const unsigned r = ln & 31;
      bf16x8 sel;
      #pragma unroll
      for (int j = 0; j < 8; ++j) sel[j] = (r == (unsigned)(8 * (j >> 2) + (j & 3)) + 4 * hh) ? (__bf16)1.0f : (__bf16)0.0f;
      asm volatile("" : "+v"(sel));
      auto await = [&](unsigned long long v, int p, int g) __attribute__((always_inline)) {
        if (!__all(v == tag)) {
          for (unsigned spins = 0; spins < (1u << 20); ++spins) {
            __builtin_amdgcn_s_sleep(4);
            if (__all(look(p, g) == tag)) break;
          }
        }
        asm volatile("" ::: "memory");
      };
      auto gather = [&](u32x4 (&v)[16], int p, int g) __attribute__((always_inline)) {
        #pragma unroll
        for (int i = 0; i < 16; ++i) v[i] = __builtin_amdgcn_raw_buffer_load_b128(rp, (p * 3 + g) * 16384 + ln * 16 + i * 1024, 0, 16);
        asm volatile("" ::: "memory");
      };
      auto finish = [&](u32x4 (&v)[16], int p, int g) __attribute__((always_inline)) {
        f32x16 a0 = f32x16{}, a1 = f32x16{};
        #pragma unroll
        for (int i = 0; i < 16; i += 2) { a0 = mfma(sel, __builtin_bit_cast(bf16x8, v[i]), a0); a1 = mfma(sel, __builtin_bit_cast(bf16x8, v[i + 1]), a1); }
        char* op = pout + ((size_t)(b * 96 + g * 32 + r) * 896 + br * 448 + 16 * p + 4 * hh) * 2;
        #pragma unroll
        for (int i = 0; i < 2; ++i) {
          u32x2 w; w[0] = cvtpk(a0[4 * i] + a1[4 * i], a0[4 * i + 1] + a1[4 * i + 1]); w[1] = cvtpk(a0[4 * i + 2] + a1[4 * i + 2], a0[4 * i + 3] + a1[4 * i + 3]);
          *(u32x2*)(op + i * 16) = w;
        }
      };
      u32x4 va4[16], vb4[16];
      await(fA, pA, gA);
      if (h == 0 && qg == 0 && ln == 0) __hip_atomic_store((GAS unsigned long long*)(pws + EPOCH_OFF + grp * 128), tag, __ATOMIC_RELAXED, __HIP_MEMORY_SCOPE_AGENT);
      gather(va4, pA, gA);
      unsigned long long fB = idx2 < 36 ? look(pB, gB) : 0ull;
      if (idx2 < 36) {
        await(fB, pB, gB);
        gather(vb4, pB, gB);
        finish(va4, pA, gA);
        finish(vb4, pB, gB);
      } else finish(va4, pA, gA);
    }
  }
}

extern "C" void kernel_launch(void* const* d_in, const int* in_sizes, int n_in, void* d_out, int out_size, void* d_ws, size_t ws_size, hipStream_t stream) {
  Params p;
  for (int br = 0; br < 2; ++br) { p.x[br] = d_in[br * 5 + 0]; p.k[br] = d_in[br * 5 + 1]; p.v[br] = d_in[br * 5 + 2]; p.wq[br] = d_in[br * 5 + 3]; p.wo[br] = d_in[br * 5 + 4]; }
  p.out = d_out; p.ws = d_ws;
  (void)hipFuncSetAttribute((const void*)fused_kernel, hipFuncAttributeMaxDynamicSharedMemorySize, LDS_BYTES);
  fused_kernel<<<256, 256, LDS_BYTES, stream>>>(p);
}
```
